```python
import torch

def _softmax(x, axis=-1):
    return torch.softmax(x, dim=axis)
def _gelu(x):
    return torch.nn.functional.gelu(x, approximate="tanh")
def _silu(x):
    return torch.nn.functional.silu(x)
def _sigmoid(x):
    return torch.sigmoid(x)
def _layernorm(x, axis=-1, eps=1e-5):
    m = x.mean(dim=axis, keepdim=True)
    v = x.var(dim=axis, keepdim=True, unbiased=False)
    return (x - m) / torch.sqrt(v + eps)
def _scatter_add(tgt, idx, src, axis=0):
    return tgt.clone().index_add_(axis, idx, src)
def _segment_sum(src, seg, n):
    out = torch.zeros((n,) + src.shape[1:], dtype=src.dtype)
    return out.index_add_(0, seg, src)
def _pad(x, pw):
    flat = tuple(v for pair in reversed(pw) for v in pair)
    return torch.nn.functional.pad(x, flat)

kernel_name = 'standalone__depthwise_separable__vision'

def setup_inputs():

    _w = (lambda a: (lambda t: t.to(torch.bfloat16) if t.is_floating_point() else t)(torch.as_tensor(a)))
    H = W = 512
    C = 192
    k = 1
    return {
        "x": _w((lambda *s: torch.randn(*s, dtype=torch.bfloat16) * 0.05)(1, H, W, C)),
        "dw": _w((lambda *s: torch.randn(*s, dtype=torch.bfloat16) * 0.05)(k, k, C)),
        "pw": _w((lambda *s: torch.randn(*s, dtype=torch.bfloat16) * 0.05)(C, C)),
    }

def reference(x, dw, pw):
    B, H, W, C = x.shape
    k = dw.shape[0]
    pad = k // 2
    xp = _pad(x, ((0,0),(pad,pad),(pad,pad),(0,0)))
    out = torch.zeros_like(x)
    for i in range(k):
        for j in range(k):
            out = out + xp[:, i:i+H, j:j+W, :] * dw[i, j][None, None, None, :]
    return torch.einsum('bhwc,cd->bhwd', out, pw)
```

```cpp
#include <hip/hip_runtime.h>
#include <hip/hip_bf16.h>

typedef unsigned int u32;
typedef __attribute__((ext_vector_type(4))) unsigned int u32x4;
typedef __attribute__((ext_vector_type(2))) unsigned int u32x2;
typedef __attribute__((ext_vector_type(4))) float f32x4;
typedef __attribute__((ext_vector_type(2))) float f32x2;
typedef __attribute__((ext_vector_type(8))) __bf16 bf16x8;
typedef __attribute__((ext_vector_type(2))) __bf16 bf16x2;

#define AUX_LD 2
#define AUX_ST 18

static constexpr int C = 192;
static constexpr int ROWB = C * 2;
static constexpr int TILE_PX = 16;
static constexpr int TILE_B = TILE_PX * ROWB;
static constexpr int SROW = 416;
static constexpr int W_BYTES = C * C * 2;
static constexpr int NWAVE = 4;
static constexpr int STAGE_B = TILE_PX * SROW;

__device__ __forceinline__ u32 pack_bf16(float a, float b) {
  f32x2 v = {a, b};
  bf16x2 r = __builtin_convertvector(v, bf16x2);
  return __builtin_bit_cast(u32, r);
}
__device__ __forceinline__ float bf_lo(u32 u) { return __builtin_bit_cast(float, u << 16); }
__device__ __forceinline__ float bf_hi(u32 u) { return __builtin_bit_cast(float, u & 0xffff0000u); }

__global__ __launch_bounds__(256, 1) void dwsep_kernel(const void* __restrict__ xp, const void* __restrict__ dwp,
                                                       const void* __restrict__ pwp, void* __restrict__ outp,
                                                       int nbytes, int ntiles) {
  __shared__ __attribute__((aligned(16))) unsigned char smem[W_BYTES + NWAVE * STAGE_B];
  const int tid = threadIdx.x;
  const int lane = tid & 63;
  const int wv = __builtin_amdgcn_readfirstlane(tid >> 6);
  const int c = lane & 15, g = lane >> 4;
  auto rx = __builtin_amdgcn_make_buffer_rsrc((void*)xp, 0, nbytes, 0x00020000);
  auto ro = __builtin_amdgcn_make_buffer_rsrc(outp, 0, nbytes, 0x00020000);
  const int gw = blockIdx.x * NWAVE + wv;
  const int GW = gridDim.x * NWAVE;

  u32x4 raw[6];
  int tile = gw;
  {
    int so = tile < ntiles ? tile * TILE_B : nbytes;
#pragma unroll
    for (int i = 0; i < 6; i++) raw[i] = __builtin_amdgcn_raw_buffer_load_b128(rx, lane * 16 + i * 1024 + so, 0, AUX_LD);
  }

  {
    const unsigned char* pw = (const unsigned char*)pwp;
#pragma unroll
    for (int jj = 0; jj < 3; jj++) {
      const int j = wv * 3 + jj;
      const int s = j >> 1, h = j & 1;
      u32x2 a[8];
      u32 b[8];
#pragma unroll
      for (int r = 0; r < 8; r++) {
        const unsigned char* row = pw + (32 * s + 8 * g + r) * ROWB;
        a[r] = *(const u32x2*)(row + c * 16 + h * 8);
        b[r] = *(const u32*)(row + 256 + c * 8 + h * 4);
      }
#pragma unroll
      for (int t = 0; t < 4; t++) {
        u32x4 f;
#pragma unroll
        for (int q = 0; q < 4; q++) {
          u32 lo = a[2 * q][t >> 1], hi = a[2 * q + 1][t >> 1];
          f[q] = (t & 1) ? __builtin_amdgcn_perm(hi, lo, 0x07060302u) : __builtin_amdgcn_perm(hi, lo, 0x05040100u);
        }
        *(u32x4*)(smem + ((h * 4 + t) * 6 + s) * 1024 + lane * 16) = f;
      }
#pragma unroll
      for (int t = 0; t < 2; t++) {
        u32x4 f;
#pragma unroll
        for (int q = 0; q < 4; q++) {
          u32 lo = b[2 * q], hi = b[2 * q + 1];
          f[q] = (t & 1) ? __builtin_amdgcn_perm(hi, lo, 0x07060302u) : __builtin_amdgcn_perm(hi, lo, 0x05040100u);
        }
        *(u32x4*)(smem + ((8 + h * 2 + t) * 6 + s) * 1024 + lane * 16) = f;
      }
    }
  }
  float dwf[3][8];
  int woff[3];
#pragma unroll
  for (int i = 0; i < 3; i++) {
    int q = 64 * i + lane;
    int px = q / 24, ch = q - px * 24;
    u32x4 d = *(const u32x4*)((const unsigned char*)dwp + ch * 16);
#pragma unroll
    for (int e = 0; e < 4; e++) { dwf[i][2 * e] = bf_lo(d[e]); dwf[i][2 * e + 1] = bf_hi(d[e]); }
    woff[i] = px * SROW + ch * 16;
  }
  unsigned char* stage = smem + W_BYTES + wv * STAGE_B;
  const unsigned char* aptr = stage + c * SROW + g * 16;
  const unsigned char* bptr = smem + lane * 16;
  const int st4 = g * 4 * ROWB + c * 16;
  const int st2 = g * 4 * ROWB + 256 + c * 8;
  __syncthreads();

  for (; tile < ntiles; tile += GW) {
#pragma unroll
    for (int i = 0; i < 6; i++) {
      u32x4 v;
#pragma unroll
      for (int e = 0; e < 4; e++)
        v[e] = pack_bf16(bf_lo(raw[i][e]) * dwf[i % 3][2 * e], bf_hi(raw[i][e]) * dwf[i % 3][2 * e + 1]);
      *(u32x4*)(stage + woff[i % 3] + (i / 3) * 8 * SROW) = v;
    }
    {
      int tn = tile + GW;
      int so = tn < ntiles ? tn * TILE_B : nbytes;
#pragma unroll
      for (int i = 0; i < 6; i++) raw[i] = __builtin_amdgcn_raw_buffer_load_b128(rx, lane * 16 + i * 1024 + so, 0, AUX_LD);
    }
    bf16x8 a[6];
#pragma unroll
    for (int s = 0; s < 6; s++) a[s] = *(const bf16x8*)(aptr + s * 64);
    f32x4 acc[12];
#pragma unroll
    for (int t = 0; t < 12; t++) {
      acc[t] = f32x4{0.f, 0.f, 0.f, 0.f};
#pragma unroll
      for (int s = 0; s < 6; s++) {
        bf16x8 b = *(const bf16x8*)(bptr + (t * 6 + s) * 1024);
        acc[t] = __builtin_amdgcn_mfma_f32_16x16x32_bf16(a[s], b, acc[t], 0, 0, 0);
      }
    }
    const int so = tile * TILE_B;
#pragma unroll
    for (int i = 0; i < 4; i++) {
      u32x4 o4 = {pack_bf16(acc[0][i], acc[1][i]), pack_bf16(acc[2][i], acc[3][i]),
                  pack_bf16(acc[4][i], acc[5][i]), pack_bf16(acc[6][i], acc[7][i])};
      u32x2 o2 = {pack_bf16(acc[8][i], acc[9][i]), pack_bf16(acc[10][i], acc[11][i])};
      __builtin_amdgcn_raw_buffer_store_b128(o4, ro, st4 + i * ROWB, so, AUX_ST);
      __builtin_amdgcn_raw_buffer_store_b64(o2, ro, st2 + i * ROWB, so, AUX_ST);
    }
  }
}

extern "C" void kernel_launch(void* const* d_in, const int* in_sizes, int n_in, void* d_out, int out_size,
                              void* d_ws, size_t ws_size, hipStream_t stream) {
  const int nbytes = in_sizes[0] * 2;
  const int ntiles = nbytes / TILE_B;
  dwsep_kernel<<<256, 256, 0, stream>>>(d_in[0], d_in[1], d_in[2], d_out, nbytes, ntiles);
}
```

```cpp
#define ORDER 2
#include <hip/hip_runtime.h>
#include <hip/hip_bf16.h>

typedef unsigned int u32;
typedef __attribute__((ext_vector_type(4))) unsigned int u32x4;
typedef __attribute__((ext_vector_type(2))) unsigned int u32x2;
typedef __attribute__((ext_vector_type(4))) float f32x4;
typedef __attribute__((ext_vector_type(2))) float f32x2;
typedef __attribute__((ext_vector_type(8))) __bf16 bf16x8;
typedef __attribute__((ext_vector_type(2))) __bf16 bf16x2;

#define AUX_LD 0
#define AUX_ST 16

#define PFD 1
#define TLBWARM 0
static constexpr int C = 192;
static constexpr int ROWB = C * 2;
static constexpr int TILE_PX = 16;
static constexpr int TILE_B = TILE_PX * ROWB;
static constexpr int SROW = 416;
static constexpr int W_BYTES = C * C * 2;
static constexpr int NWAVE = 4;
static constexpr int STAGE_B = TILE_PX * SROW;

__device__ __forceinline__ u32 pack_bf16(float a, float b) {
  f32x2 v = {a, b};
  bf16x2 r = __builtin_convertvector(v, bf16x2);
  return __builtin_bit_cast(u32, r);
}
__device__ __forceinline__ float bf_lo(u32 u) { return __builtin_bit_cast(float, u << 16); }
__device__ __forceinline__ float bf_hi(u32 u) { return __builtin_bit_cast(float, u & 0xffff0000u); }

__global__ __launch_bounds__(256, 1) void dwsep_kernel(const void* __restrict__ xp, const void* __restrict__ dwp,
                                                       const void* __restrict__ pwp, void* __restrict__ outp,
                                                       int nbytes, int ntiles) {
  __shared__ __attribute__((aligned(16))) unsigned char smem[W_BYTES + NWAVE * STAGE_B];
  const int tid = threadIdx.x;
  const int lane = tid & 63;
  const int wv = __builtin_amdgcn_readfirstlane(tid >> 6);
  const int c = lane & 15, g = lane >> 4;
  auto rx = __builtin_amdgcn_make_buffer_rsrc((void*)xp, 0, nbytes, 0x00020000);
  auto ro = __builtin_amdgcn_make_buffer_rsrc(outp, 0, nbytes, 0x00020000);
  const int gw = blockIdx.x * NWAVE + wv;
  const int GW = gridDim.x * NWAVE;

  const int nit = ntiles / GW;
  auto tile_of = [&](int it) -> int {
    int b = (blockIdx.x & 7) * (gridDim.x >> 3) + (blockIdx.x >> 3);
    int per = gridDim.x >> 3;
    int xc = b / per, bi = b - xc * per;
    return (xc * nit + it) * per * NWAVE + bi * NWAVE + wv;
  };
  u32x4 raw[PFD][6];
#pragma unroll
  for (int d = 0; d < PFD; d++) {
    int so = tile_of(d) * TILE_B;
#pragma unroll
    for (int i = 0; i < 6; i++) raw[d][i] = __builtin_amdgcn_raw_buffer_load_b128(rx, lane * 16 + i * 1024 + so, 0, AUX_LD);
  }

  {
    const unsigned char* pw = (const unsigned char*)pwp;
#pragma unroll
    for (int jj = 0; jj < 3; jj++) {
      const int j = wv * 3 + jj;
      const int s = j >> 1, h = j & 1;
      u32x2 a[8];
      u32 b[8];
#pragma unroll
      for (int r = 0; r < 8; r++) {
        const unsigned char* row = pw + (32 * s + 8 * g + r) * ROWB;
        a[r] = *(const u32x2*)(row + c * 16 + h * 8);
        b[r] = *(const u32*)(row + 256 + c * 8 + h * 4);
      }
#pragma unroll
      for (int t = 0; t < 4; t++) {
        u32x4 f;
#pragma unroll
        for (int q = 0; q < 4; q++) {
          u32 lo = a[2 * q][t >> 1], hi = a[2 * q + 1][t >> 1];
          f[q] = (t & 1) ? __builtin_amdgcn_perm(hi, lo, 0x07060302u) : __builtin_amdgcn_perm(hi, lo, 0x05040100u);
        }
        *(u32x4*)(smem + ((h * 4 + t) * 6 + s) * 1024 + lane * 16) = f;
      }
#pragma unroll
      for (int t = 0; t < 2; t++) {
        u32x4 f;
#pragma unroll
        for (int q = 0; q < 4; q++) {
          u32 lo = b[2 * q], hi = b[2 * q + 1];
          f[q] = (t & 1) ? __builtin_amdgcn_perm(hi, lo, 0x07060302u) : __builtin_amdgcn_perm(hi, lo, 0x05040100u);
        }
        *(u32x4*)(smem + ((8 + h * 2 + t) * 6 + s) * 1024 + lane * 16) = f;
      }
    }
  }
  float dwf[3][8];
  int woff[3];
#pragma unroll
  for (int i = 0; i < 3; i++) {
    int q = 64 * i + lane;
    int px = q / 24, ch = q - px * 24;
    u32x4 d = *(const u32x4*)((const unsigned char*)dwp + ch * 16);
#pragma unroll
    for (int e = 0; e < 4; e++) { dwf[i][2 * e] = bf_lo(d[e]); dwf[i][2 * e + 1] = bf_hi(d[e]); }
    woff[i] = px * SROW + ch * 16;
  }
  unsigned char* stage = smem + W_BYTES + wv * STAGE_B;
  const unsigned char* aptr = stage + c * SROW + g * 16;
  const unsigned char* bptr = smem + lane * 16;
  const int st4 = g * 4 * ROWB + c * 16;
  const int st2 = g * 4 * ROWB + 256 + c * 8;
  __syncthreads();
  bf16x8 wf[12][6];
#pragma unroll
  for (int t = 0; t < 12; t++)
#pragma unroll
    for (int s = 0; s < 6; s++) wf[t][s] = *(const bf16x8*)(bptr + (t * 6 + s) * 1024);
  __builtin_amdgcn_sched_barrier(0);

  for (int it0 = 0; it0 < nit; it0 += PFD) {
#pragma unroll
   for (int d = 0; d < PFD; d++) {
    const int it = it0 + d;
    const int tile = tile_of(it);
    const int sn = it + PFD < nit ? tile_of(it + PFD) * TILE_B : nbytes;
#pragma unroll
    for (int i = 0; i < 6; i++) {
      u32x4 v;
#pragma unroll
      for (int e = 0; e < 4; e++)
        v[e] = pack_bf16(bf_lo(raw[d][i][e]) * dwf[i % 3][2 * e], bf_hi(raw[d][i][e]) * dwf[i % 3][2 * e + 1]);
      *(u32x4*)(stage + woff[i % 3] + (i / 3) * 8 * SROW) = v;
      raw[d][i] = __builtin_amdgcn_raw_buffer_load_b128(rx, lane * 16 + i * 1024 + sn, 0, AUX_LD);
      __builtin_amdgcn_sched_barrier(0);
    }
    bf16x8 a[6];
#pragma unroll
    for (int s = 0; s < 6; s++) a[s] = *(const bf16x8*)(aptr + s * 64);
    __builtin_amdgcn_sched_barrier(0);
    f32x4 acc[12];
#pragma unroll
    for (int t = 0; t < 12; t++) acc[t] = f32x4{0.f, 0.f, 0.f, 0.f};
#pragma unroll
    for (int s = 0; s < 6; s++)
#pragma unroll
      for (int t = 0; t < 12; t++)
        acc[t] = __builtin_amdgcn_mfma_f32_16x16x32_bf16(a[s], wf[t][s], acc[t], 0, 0, 0);
    const int so = tile * TILE_B;
#pragma unroll
    for (int i = 0; i < 4; i++) {
      u32x4 o4 = {pack_bf16(acc[0][i], acc[1][i]), pack_bf16(acc[2][i], acc[3][i]),
                  pack_bf16(acc[4][i], acc[5][i]), pack_bf16(acc[6][i], acc[7][i])};
      u32x2 o2 = {pack_bf16(acc[8][i], acc[9][i]), pack_bf16(acc[10][i], acc[11][i])};
      __builtin_amdgcn_raw_buffer_store_b128(o4, ro, st4 + i * ROWB, so, AUX_ST);
      __builtin_amdgcn_raw_buffer_store_b64(o2, ro, st2 + i * ROWB, so, AUX_ST);
    }
    __builtin_amdgcn_sched_barrier(0);
   }
  }
}

extern "C" void kernel_launch(void* const* d_in, const int* in_sizes, int n_in, void* d_out, int out_size,
                              void* d_ws, size_t ws_size, hipStream_t stream) {
  const int nbytes = in_sizes[0] * 2;
  const int ntiles = nbytes / TILE_B;
  dwsep_kernel<<<256, 256, 0, stream>>>(d_in[0], d_in[1], d_in[2], d_out, nbytes, ntiles);
}
```

```cpp
#include <hip/hip_runtime.h>
#include <hip/hip_bf16.h>

typedef unsigned int u32;
typedef __attribute__((ext_vector_type(4))) unsigned int u32x4;
typedef __attribute__((ext_vector_type(2))) unsigned int u32x2;
typedef __attribute__((ext_vector_type(4))) float f32x4;
typedef __attribute__((ext_vector_type(2))) float f32x2;
typedef __attribute__((ext_vector_type(8))) __bf16 bf16x8;
typedef __attribute__((ext_vector_type(2))) __bf16 bf16x2;

#define AUX_LD 0
#define AUX_ST 16

#define CPF 1
#define CPFLEN 5000
#define DUMMY 1
#define NEV 2176
#define PFD 1
static constexpr int C = 192;
static constexpr int ROWB = C * 2;
static constexpr int TILE_PX = 16;
static constexpr int TILE_B = TILE_PX * ROWB;
static constexpr int SROW = 416;
static constexpr int W_BYTES = C * C * 2;
static constexpr int NWAVE = 4;
static constexpr int STAGE_B = TILE_PX * SROW;

__device__ __forceinline__ u32 pack_bf16(float a, float b) {
  f32x2 v = {a, b};
  bf16x2 r = __builtin_convertvector(v, bf16x2);
  return __builtin_bit_cast(u32, r);
}
__device__ __forceinline__ float bf_lo(u32 u) { return __builtin_bit_cast(float, u << 16); }
__device__ __forceinline__ float bf_hi(u32 u) { return __builtin_bit_cast(float, u & 0xffff0000u); }

__global__ __launch_bounds__(256, 1) void dwsep_kernel(const void* __restrict__ xp, const void* __restrict__ dwp,
                                                       const void* __restrict__ pwp, void* __restrict__ outp,
                                                       int nbytes, int ntiles) {
  __shared__ __attribute__((aligned(16))) unsigned char smem[W_BYTES + NWAVE * STAGE_B];
  const int tid = threadIdx.x;
  const int lane = tid & 63;
  const int wv = __builtin_amdgcn_readfirstlane(tid >> 6);
  const int c = lane & 15, g = lane >> 4;
  u32 cpf;
  {
    unsigned long long pc = __builtin_amdgcn_s_getpc();
    auto rc = __builtin_amdgcn_make_buffer_rsrc((void*)pc, 0, (CPF == 2 || wv == 0) ? CPFLEN : 0, 0x00020000);
    cpf = __builtin_amdgcn_raw_buffer_load_b32(rc, lane * 128, 0, 0);
    __builtin_amdgcn_sched_barrier(0);
  }
  auto rx = __builtin_amdgcn_make_buffer_rsrc((void*)xp, 0, nbytes, 0x00020000);
  auto ro = __builtin_amdgcn_make_buffer_rsrc(outp, 0, nbytes, 0x00020000);

  const int xc = blockIdx.x & 7;
  const int pairn = ntiles >> 2;
  const int nev = (int)(((long long)pairn * NEV) >> 12);
  const int tbeg = (xc >> 1) * pairn + ((xc & 1) ? nev : 0) + (blockIdx.x >> 3) * NWAVE + wv;
  const int tend = (xc >> 1) * pairn + ((xc & 1) ? pairn : nev);
  const int nit = (tend - tbeg + 127) >> 7;
  auto tile_of = [&](int it) -> int { return tbeg + it * 128; };
  asm volatile("" :: "s"(outp));
  u32x4 wa[3][4];
  u32x2 wb[3][4];
  auto rw = __builtin_amdgcn_make_buffer_rsrc((void*)pwp, 0, W_BYTES, 0x00020000);
  {
    const int vo = 8 * g * ROWB + c * 16, vo2 = 8 * g * ROWB + 256 + c * 8;
#pragma unroll
    for (int jj = 0; jj < 3; jj++) {
      const int j = wv * 3 + jj;
      const int so = ((j >> 1) * 32 + (j & 1) * 4) * ROWB;
#pragma unroll
      for (int r = 0; r < 4; r++) {
        wa[jj][r] = __builtin_amdgcn_raw_buffer_load_b128(rw, vo + r * ROWB, so, 0);
        wb[jj][r] = __builtin_amdgcn_raw_buffer_load_b64(rw, vo2 + r * ROWB, so, 0);
      }
    }
  }
  u32x4 dwr[3];
  int woff[3];
  auto rd = __builtin_amdgcn_make_buffer_rsrc((void*)dwp, 0, ROWB, 0x00020000);
#pragma unroll
  for (int i = 0; i < 3; i++) {
    int q = 64 * i + lane;
    int px = q / 24, ch = q - px * 24;
    dwr[i] = __builtin_amdgcn_raw_buffer_load_b128(rd, ch * 16, 0, 0);
    woff[i] = px * SROW + ch * 16;
  }
  __builtin_amdgcn_sched_barrier(0);
  u32x4 raw[PFD][6];
#pragma unroll
  for (int d = 0; d < PFD; d++) {
    int so = tile_of(d) * TILE_B;
#pragma unroll
    for (int i = 0; i < 6; i++) raw[d][i] = __builtin_amdgcn_raw_buffer_load_b128(rx, lane * 16 + i * 1024 + so, 0, AUX_LD);
    __builtin_amdgcn_sched_barrier(0);
  }
  __builtin_amdgcn_sched_barrier(0);
#pragma unroll
  for (int jj = 0; jj < 3; jj++) {
    const int j = wv * 3 + jj;
    unsigned char* dst = smem + (j >> 1) * 1024 + (j & 1) * 8 + lane * 16;
#pragma unroll
    for (int t = 0; t < 12; t++) {
      u32x2 f;
#pragma unroll
      for (int q = 0; q < 2; q++) {
        u32 lo = t < 8 ? wa[jj][2 * q][t >> 1] : wb[jj][2 * q][(t - 8) >> 1];
        u32 hi = t < 8 ? wa[jj][2 * q + 1][t >> 1] : wb[jj][2 * q + 1][(t - 8) >> 1];
        f[q] = (t & 1) ? __builtin_amdgcn_perm(hi, lo, 0x07060302u) : __builtin_amdgcn_perm(hi, lo, 0x05040100u);
      }
      *(u32x2*)(dst + t * 6144) = f;
    }
  }
  asm volatile("" :: "v"(cpf));
  float dwf[3][8];
#pragma unroll
  for (int i = 0; i < 3; i++)
#pragma unroll
    for (int e = 0; e < 4; e++) { dwf[i][2 * e] = bf_lo(dwr[i][e]); dwf[i][2 * e + 1] = bf_hi(dwr[i][e]); }
  unsigned char* stage = smem + W_BYTES + wv * STAGE_B;
  const unsigned char* aptr = stage + c * SROW + g * 16;
  const unsigned char* bptr = smem + lane * 16;
  const int st4 = g * 4 * ROWB + c * 16;
  const int st2 = g * 4 * ROWB + 256 + c * 8;
  __syncthreads();
  bf16x8 wf[12][6];
#pragma unroll
  for (int s = 0; s < 6; s++)
#pragma unroll
    for (int t = 0; t < 12; t++) wf[t][s] = *(const bf16x8*)(bptr + (t * 6 + s) * 1024);
  __builtin_amdgcn_sched_barrier(0);
#pragma unroll
  for (int i = 0; i < 8; i++) __builtin_amdgcn_raw_buffer_store_b32(0u, ro, nbytes + lane * 4 + i * 256, 0, 0);
  __builtin_amdgcn_sched_barrier(0);

  for (int it0 = 0; it0 < nit; it0 += PFD) {
#pragma unroll
   for (int d = 0; d < PFD; d++) {
    const int it = it0 + d;
    const int tile = tile_of(it);
    const int sn = it + PFD < nit ? tile_of(it + PFD) * TILE_B : nbytes;
#pragma unroll
    for (int i = 0; i < 6; i++) {
      u32x4 v;
#pragma unroll
      for (int e = 0; e < 4; e++)
        v[e] = pack_bf16(bf_lo(raw[d][i][e]) * dwf[i % 3][2 * e], bf_hi(raw[d][i][e]) * dwf[i % 3][2 * e + 1]);
      *(u32x4*)(stage + woff[i % 3] + (i / 3) * 8 * SROW) = v;
      raw[d][i] = __builtin_amdgcn_raw_buffer_load_b128(rx, lane * 16 + i * 1024 + sn, 0, AUX_LD);
      __builtin_amdgcn_sched_barrier(0);
    }
    bf16x8 a[6];
#pragma unroll
    for (int s = 0; s < 6; s++) a[s] = *(const bf16x8*)(aptr + s * 64);
    __builtin_amdgcn_sched_barrier(0);
    f32x4 acc[12];
#pragma unroll
    for (int s = 0; s < 6; s++)
#pragma unroll
      for (int t = 0; t < 12; t++) {
        if (s == 0) {
          if (t < 8) asm volatile("v_mfma_f32_16x16x32_bf16 %0, %1, %2, 0" : "=&v"(acc[t]) : "v"(a[s]), "v"(wf[t][s]));
          else asm volatile("v_mfma_f32_16x16x32_bf16 %0, %1, %2, 0" : "=&v"(acc[t]) : "v"(a[s]), "a"(wf[t][s]));
        } else {
          asm volatile("v_mfma_f32_16x16x32_bf16 %0, %1, %2, %0" : "+v"(acc[t]) : "v"(a[s]), "a"(wf[t][s]));
        }
      }
    asm volatile("s_nop 7\n\ts_nop 1" ::: "memory");
    __builtin_amdgcn_sched_barrier(0);
    const int so = tile * TILE_B;
#pragma unroll
    for (int i = 0; i < 4; i++) {
      u32x4 o4 = {pack_bf16(acc[0][i], acc[1][i]), pack_bf16(acc[2][i], acc[3][i]),
                  pack_bf16(acc[4][i], acc[5][i]), pack_bf16(acc[6][i], acc[7][i])};
      u32x2 o2 = {pack_bf16(acc[8][i], acc[9][i]), pack_bf16(acc[10][i], acc[11][i])};
      __builtin_amdgcn_raw_buffer_store_b128(o4, ro, st4 + i * ROWB, so, AUX_ST);
      __builtin_amdgcn_raw_buffer_store_b64(o2, ro, st2 + i * ROWB, so, AUX_ST);
    }
    __builtin_amdgcn_sched_barrier(0);
   }
  }
}

extern "C" void kernel_launch(void* const* d_in, const int* in_sizes, int n_in, void* d_out, int out_size,
                              void* d_ws, size_t ws_size, hipStream_t stream) {
  const int nbytes = in_sizes[0] * 2;
  const int ntiles = nbytes / TILE_B;
  dwsep_kernel<<<256, 256, 0, stream>>>(d_in[0], d_in[1], d_in[2], d_out, nbytes, ntiles);
}
```
